```python
import torch

def _softmax(x, axis=-1):
    return torch.softmax(x, dim=axis)
def _gelu(x):
    return torch.nn.functional.gelu(x, approximate="tanh")
def _silu(x):
    return torch.nn.functional.silu(x)
def _sigmoid(x):
    return torch.sigmoid(x)
def _layernorm(x, axis=-1, eps=1e-5):
    m = x.mean(dim=axis, keepdim=True)
    v = x.var(dim=axis, keepdim=True, unbiased=False)
    return (x - m) / torch.sqrt(v + eps)
def _scatter_add(tgt, idx, src, axis=0):
    return tgt.clone().index_add_(axis, idx, src)
def _segment_sum(src, seg, n):
    out = torch.zeros((n,) + src.shape[1:], dtype=src.dtype)
    return out.index_add_(0, seg, src)
def _pad(x, pw):
    flat = tuple(v for pair in reversed(pw) for v in pair)
    return torch.nn.functional.pad(x, flat)

kernel_name = 'recurrent_scan__sliding_block_cached__dense_activation_gelu__decode'

def setup_inputs():

    _w = (lambda a: (lambda t: t.to(torch.bfloat16) if t.is_floating_point() else t)(torch.as_tensor(a)))
    def _A_setup():
        B, Sq, D = 1, 1, 640
        Hq, Hkv, Sk, Dh = 32, 32, 4096, 128
        assert Hq % Hkv == 0, (Hq, Hkv)
        return {
            "x": _w((lambda *s: torch.randn(*s, dtype=torch.bfloat16) * 0.05)(B, Sq, D)),
            "k_cache": _w((lambda *s: torch.randn(*s, dtype=torch.bfloat16) * 3.2)(B, Hkv, Sk, Dh)),
            "v_cache": _w((lambda *s: torch.randn(*s, dtype=torch.bfloat16) * 3.2)(B, Hkv, Sk, Dh)),
            "wq": _w((lambda *s: torch.randn(*s, dtype=torch.bfloat16) * 3.2)(D, Hq * Dh) * (D ** -0.5)),
            "wo": _w((lambda *s: torch.randn(*s, dtype=torch.bfloat16) * 3.2)(Hq * Dh, D) * ((Hq * Dh) ** -0.5)),
        }
    _A_in = _A_setup()

    def _B_setup():
        B, S, D, F = 1, 1, 640, 1728
        return {
            "x": _w((lambda *s: torch.randn(*s, dtype=torch.bfloat16) * 0.05)(B, S, D)),
            "w1": _w((lambda *s: torch.randn(*s, dtype=torch.bfloat16) * 3.2)(D, F) * (D ** -0.5)),
            "b1": _w((lambda *s: torch.randn(*s, dtype=torch.bfloat16) * 3.2)(F) * 0.01),
            "w2": _w((lambda *s: torch.randn(*s, dtype=torch.bfloat16) * 0.4)(F, D) * (F ** -0.5)),
            "b2": _w((lambda *s: torch.randn(*s, dtype=torch.bfloat16) * 0.4)(D) * 0.01),
        }
    _B_in = _B_setup()

    return {
        "A_x": _A_in["x"],
        "A_k_cache": _A_in["k_cache"],
        "A_v_cache": _A_in["v_cache"],
        "A_wq": _A_in["wq"],
        "A_wo": _A_in["wo"],
        "B_x": _B_in["x"],
        "B_w1": _B_in["w1"],
        "B_b1": _B_in["b1"],
        "B_w2": _B_in["w2"],
        "B_b2": _B_in["b2"],
    }

def reference(A_x, A_k_cache, A_v_cache, A_wq, A_wo, B_x, B_w1, B_b1, B_w2, B_b2):
    def _A_ref(x, k_cache, v_cache, wq, wo):
        B, Sq, D = x.shape
        Hq, Dh = 32, 128
        Hkv, Sk = k_cache.shape[1], k_cache.shape[2]
        W = min(4096, max(256, Sk // 4))
        rep = Hq // Hkv
        q = (lambda x, *d: x.permute(*d))((x @ wq).reshape(B, Sq, Hq, Dh), 0, 2, 1, 3)
        k = (lambda x, n, axis: torch.repeat_interleave(x, n, dim=axis))(k_cache[:, :, Sk - W:, :], rep, axis=1)
        v = (lambda x, n, axis: torch.repeat_interleave(x, n, dim=axis))(v_cache[:, :, Sk - W:, :], rep, axis=1)
        scores = torch.einsum("bhqd,bhkd->bhqk", q, k) * (Dh ** -0.5)
        p = _softmax(scores, axis=-1)
        o = torch.einsum("bhqk,bhkd->bhqd", p, v)
        return (lambda x, *d: x.permute(*d))(o, 0, 2, 1, 3).reshape(B, Sq, Hq * Dh) @ wo

    def _B_ref(x, w1, b1, w2, b2):
        h = torch.einsum("bsd,df->bsf", x, w1) + b1
        h = _gelu(h)
        return torch.einsum("bsf,fd->bsd", h, w2) + b2

    x = A_x
    xn = _layernorm(x, axis=-1)
    s = _A_ref(xn, A_k_cache, A_v_cache, A_wq, A_wo)
    g = _silu(_B_ref(xn, B_w1, B_b1, B_w2, B_b2))
    return x + s * g
```

```cpp
#include <hip/hip_runtime.h>
#include <hip/hip_bf16.h>
#include <stdint.h>

typedef unsigned short u16;
typedef unsigned int u32;
typedef unsigned long long u64;
typedef u32 v16u __attribute__((ext_vector_type(16)));
typedef u32 v8u __attribute__((ext_vector_type(8)));
typedef u32 v4u __attribute__((ext_vector_type(4)));
typedef u32 v2u __attribute__((ext_vector_type(2)));
typedef short v4s __attribute__((ext_vector_type(4)));
typedef float f32x4 __attribute__((ext_vector_type(4)));
typedef __bf16 bf16x8 __attribute__((ext_vector_type(8)));
typedef __bf16 bf16x2 __attribute__((ext_vector_type(2)));
typedef float f32x2 __attribute__((ext_vector_type(2)));
typedef __amdgpu_buffer_rsrc_t rsrc_t;
#define LAS __attribute__((address_space(3)))
#define CAS __attribute__((address_space(4)))

#define STEP 64
#define L_T0 55
#define SLACK 640
#define Q_S1 4
#define Q_S2 20
#define O_SLEEP 8
#define F_DELAY 7
#define E3_DELAY 4
#define E3P 4
#define H_SLEEP 4
#define H_AT 8
#define RWAIT 16
#define PDELAY 4
constexpr int AUX_SC1 = 16;

constexpr int D = 640, F = 1728;
constexpr int NWAVES = 10, NTHR = NWAVES * 64;

constexpr u32 L_ST = 96, L_Z = 128  , L_Q = 256, L_O = L_Q  , L_P = 512, L_HS = 1024, L_XN = 1536, L_JUNK = 4096;
constexpr u32 VP = 1056;
constexpr u32 L_W1 = 5120, L_K = L_W1 + 10240, L_V = L_K + 32768, L_W2 = L_V + 32 * VP, L_WO = L_W2 + 8192, L_END = L_WO + 20480;
static_assert(L_K == L_W1 + 10240 && L_V == L_W1 + 43008 && L_W2 == L_W1 + 76800 && L_WO == L_W1 + 84992, "offsets used by the loader");
constexpr u32 FL_XN = 0, FL_TAG = 4, FL_Q = 8, FL_G = 12, FL_P = 16  , FL_S1 = 24, FL_SK = 28, FL_SV = 32, FL_S2 = 36, FL_SO = 40, FL_O = 48  , NXO = 36, FL_POL = 80  ;
constexpr u32 W_EP = 0, W_QX = 4096, W_HX = W_QX + 32 * 4 * 128 * 8, W_PF = W_HX + 8 * F * 8, W_OX = 0x60000, OREC = 1024;
static_assert(W_PF + 27 * 640 * 8 <= W_OX, "");
constexpr u32 W_PS = W_OX + 256 * OREC, W_RACE = W_PS + 20 * 16384, W_END = W_RACE + 32768, W_POL = 2048;
static_assert(W_PS % 16384 == 0, "");
constexpr int SPIN_MAX = 1 << 21;
constexpr u32 OOB = 0x40000000u;

__device__ __forceinline__ float bflo(u32 v) { return __uint_as_float(v << 16); }
__device__ __forceinline__ float bfhi(u32 v) { return __uint_as_float(v & 0xffff0000u); }
__device__ __forceinline__ u32 cvtpk(float lo, float hi) {
  f32x2 v = {lo, hi};
  return __builtin_bit_cast(u32, __builtin_convertvector(v, bf16x2));
}
__device__ __forceinline__ float rbf(float f) { return bflo(cvtpk(f, f)); }
#define U2F __uint_as_float
#define F2U __float_as_uint
template <int CTRL> __device__ __forceinline__ float dpp(float x) {
  return __builtin_bit_cast(float, __builtin_amdgcn_mov_dpp(__builtin_bit_cast(int, x), CTRL, 0xf, 0xf, true));
}
__device__ __forceinline__ float fmx(float a, float b) { float r; asm("v_max_f32 %0, %1, %2" : "=v"(r) : "v"(a), "v"(b)); return r; }
constexpr int XOR1 = 0xB1, XOR2 = 0x4E, HMIR = 0x141, ROR4 = 0x124, ROR8 = 0x128;
__device__ __forceinline__ float fold32(float a, float b) { auto s = __builtin_amdgcn_permlane32_swap(F2U(a), F2U(b), false, false); return U2F(s[0]) + U2F(s[1]); }
__device__ __forceinline__ float fold16(float a, float b) { auto s = __builtin_amdgcn_permlane16_swap(F2U(a), F2U(b), false, false); return U2F(s[0]) + U2F(s[1]); }
__device__ __forceinline__ float x16_max(float x) { auto s = __builtin_amdgcn_permlane16_swap(F2U(x), F2U(x), false, false); return fmx(U2F(s[0]), U2F(s[1])); }
__device__ __forceinline__ float x32_max(float x) { auto s = __builtin_amdgcn_permlane32_swap(F2U(x), F2U(x), false, false); return fmx(U2F(s[0]), U2F(s[1])); }
__device__ __forceinline__ float wave_sum(float x) { x += dpp<XOR1>(x); x += dpp<XOR2>(x); x += dpp<HMIR>(x); x += dpp<ROR8>(x); x = fold16(x, x); return fold32(x, x); }
#define DPPMAX(ctl) "s_nop 1\n\tv_max_f32_dpp %0, %0, %0 " ctl " row_mask:0xf bank_mask:0xf\n\t"
__device__ __forceinline__ float wave_max(float x) {
  asm(DPPMAX("quad_perm:[1,0,3,2]") DPPMAX("quad_perm:[2,3,0,1]") DPPMAX("row_half_mirror") DPPMAX("row_ror:8") "s_nop 0" : "+v"(x));
  return x32_max(x16_max(x));
}
__device__ __forceinline__ float fexp(float x) { return __builtin_amdgcn_exp2f(x * 1.4426950408889634f); }
__device__ __forceinline__ f32x4 mfma(v4u a, v4u b, f32x4 c) {
  return __builtin_amdgcn_mfma_f32_16x16x32_bf16(__builtin_bit_cast(bf16x8, a), __builtin_bit_cast(bf16x8, b), c, 0, 0, 0);
}
#define CBAR() asm volatile("" ::: "memory")
#define SB() __builtin_amdgcn_sched_barrier(0)
#define PINV(x) asm volatile("" : "+v"(x))
#define PINS(x) asm volatile("" : "+s"(x))

__device__ __forceinline__ rsrc_t mk(const void* p, u32 bytes) { return __builtin_amdgcn_make_buffer_rsrc((void*)p, 0, bytes, 0x00020000); }
__device__ __forceinline__ v4u ldx(rsrc_t r, u32 voff, u32 soff) { return __builtin_amdgcn_raw_buffer_load_b128(r, voff, soff, AUX_SC1); }
__device__ __forceinline__ bool all_zero(u32 v) { PINV(v); return __builtin_amdgcn_ballot_w64(v != 0) == 0; }
__device__ __forceinline__ u32 tdiff(v4u g, u32 tag) { return (g.y ^ tag) | (g.w ^ tag); }

template <typename T> __device__ __forceinline__ T lds_ld(u32 a) { return *(LAS T*)(uintptr_t)a; }
template <typename T> __device__ __forceinline__ void lds_st(u32 a, T v) { *(LAS T*)(uintptr_t)a = v; }
template <u32 A> __device__ __forceinline__ void lds_set(u32 base) { u32 one = 1; asm volatile("ds_write_b32 %0, %1 offset:%2" :: "v"(base), "v"(one), "n"(A) : "memory"); }
#define WAIT_END "s_sub_u32 %[n], %[n], 1\n\ts_waitcnt lgkmcnt(0)\n\tv_cmp_gt_i32 vcc, %[need], %[t]\n\ts_cbranch_scc1 3f\n\ts_cbranch_vccnz 1b\n3:"
template <int SLEEP, u32 A> __device__ __forceinline__ void lds_wait(u32& n, u32 base, int need) {
  u32 t;
  asm volatile("1:\n\ts_sleep %[sl]\n\tds_read_b32 %[t], %[fb] offset:%[fa]\n\t" WAIT_END
               : [t] "=&v"(t), [n] "+s"(n) : [fb] "v"(base), [need] "s"(need), [sl] "n"(SLEEP), [fa] "n"(A) : "memory", "vcc", "scc");
}
#define SPEC_BEGIN "1:\n\tds_read_b32 %[t], %[fb] offset:%[fa]\n\t"
template <u32 FA, u32 O0, u32 O1, u32 O2, u32 O3>
__device__ __forceinline__ void lds_wait_rd4(u32& n, u32 fb, int need, u32 a, u32 a2, v4u& d0, v4u& d1, v4u& d2, v4u& d3) {
  u32 t;
  asm volatile(SPEC_BEGIN "ds_read_b128 %[d0], %[a] offset:%[o0]\n\tds_read_b128 %[d1], %[a] offset:%[o1]\n\tds_read_b128 %[d2], %[a2] offset:%[o2]\n\tds_read_b128 %[d3], %[a2] offset:%[o3]\n\t" WAIT_END
               : [t] "=&v"(t), [n] "+s"(n), [d0] "=&v"(d0), [d1] "=&v"(d1), [d2] "=&v"(d2), [d3] "=&v"(d3)
               : [fb] "v"(fb), [fa] "n"(FA), [need] "s"(need), [a] "v"(a), [a2] "v"(a2), [o0] "n"(O0), [o1] "n"(O1), [o2] "n"(O2), [o3] "n"(O3) : "memory", "vcc", "scc");
}
template <int SLEEP> __device__ __forceinline__ u32 lds_wait_tag(u32& n, u32 fb) {
  u32 t, d; const int need = 1;
  asm volatile("1:\n\ts_sleep %[sl]\n\tds_read_b32 %[t], %[fb] offset:%[fa]\n\tds_read_b32 %[d], %[fb] offset:%[o]\n\t" WAIT_END
               : [t] "=&v"(t), [n] "+s"(n), [d] "=&v"(d) : [fb] "v"(fb), [fa] "n"(FL_XN), [need] "s"(need), [o] "n"(FL_TAG), [sl] "n"(SLEEP) : "memory", "vcc", "scc");
  return d;
}
__device__ __forceinline__ u32 lds_wait_pol(u32& n, u32 fb) {
  u32 t; const int need = 1;
  asm volatile("1:\n\ts_sleep 4\n\tds_read_b32 %[t], %[fb] offset:%[fa]\n\t" WAIT_END
               : [t] "=&v"(t), [n] "+s"(n) : [fb] "v"(fb), [fa] "n"(FL_POL), [need] "s"(need) : "memory", "vcc", "scc");
  return __builtin_amdgcn_readfirstlane(t) & 8192u;
}
#define TR(i, off) "ds_read_b64_tr_b16 %[d" #i "], %[a] offset:%[o]+" #off "\n\t"
template <u32 O>
__device__ __forceinline__ void lds_wait_tr8(u32& n, u32 fb, int need, u32 a, v2u (&d)[8]) {
  u32 t;
  asm volatile(SPEC_BEGIN TR(0, 0) TR(1, 256) TR(2, 32) TR(3, 288) TR(4, 64) TR(5, 320) TR(6, 96) TR(7, 352) WAIT_END
               : [t] "=&v"(t), [n] "+s"(n), [d0] "=&v"(d[0]), [d1] "=&v"(d[1]), [d2] "=&v"(d[2]), [d3] "=&v"(d[3]), [d4] "=&v"(d[4]), [d5] "=&v"(d[5]), [d6] "=&v"(d[6]), [d7] "=&v"(d[7])
               : [fb] "v"(fb), [fa] "n"(FL_SV), [need] "s"(need), [a] "v"(a), [o] "n"(O) : "memory", "vcc", "scc");
}


__device__ __forceinline__ void publish64(f32x4 d, rsrc_t r, u32 voff, u32 soff, u32 tag) {
  auto s0 = __builtin_amdgcn_permlane32_swap(F2U(d[0]), F2U(d[2]), false, false);
  auto s1 = __builtin_amdgcn_permlane32_swap(F2U(d[1]), F2U(d[3]), false, false);
  const float v0 = U2F(s0[0]) + dpp<ROR8>(U2F(s0[1]));
  const float v1 = U2F(s1[0]) + dpp<ROR8>(U2F(s1[1]));
  __builtin_amdgcn_raw_buffer_store_b128(v4u{F2U(v0), tag, F2U(v1), tag}, r, voff, soff, AUX_SC1);
}
template <int MODE, int VM> __device__ __forceinline__ void gstep(f32x4& acc, const v4u& a, v4u& b, v4u& an) {
  if (MODE == 1) asm volatile("s_waitcnt vmcnt(%4)\n\ts_nop 0\n\tv_mfma_f32_16x16x32_bf16 %0, %3, %2, 0" : "=&v"(acc), "+v"(an), "+v"(b) : "v"(a), "n"(VM));
  else if (MODE == 2) asm volatile("s_waitcnt vmcnt(%3)\n\tv_mfma_f32_16x16x32_bf16 %0, %2, %1, %0\n\ts_nop 7" : "+v"(acc), "+v"(b) : "v"(a), "n"(VM));
  else asm volatile("s_waitcnt vmcnt(%4)\n\tv_mfma_f32_16x16x32_bf16 %0, %3, %2, %0" : "+v"(acc), "+v"(an), "+v"(b) : "v"(a), "n"(VM));
}
#define T8(p) "s_load_dword %0, " p ", 0x0\n\ts_load_dword %0, " p ", 0x40\n\ts_load_dword %0, " p ", 0x80\n\ts_load_dword %0, " p ", 0xc0\n\ts_load_dword %0, " p ", 0x100\n\t" \
              "s_load_dword %0, " p ", 0x140\n\ts_load_dword %0, " p ", 0x180\n\ts_load_dword %0, " p ", 0x1c0\n\t"
__device__ __forceinline__ void stouch8(const void* p) { u32 t0; asm volatile(T8("%1") "s_waitcnt lgkmcnt(0)" : "=&s"(t0) : "s"(p) : "memory"); }
__device__ __forceinline__ void stouch4(const void* p, const void* q) { u32 t0; asm volatile("s_load_dword %0, %1, 0x0\n\ts_load_dword %0, %1, 0x40\n\ts_load_dword %0, %2, 0x0\n\ts_load_dword %0, %2, 0x40\n\ts_waitcnt lgkmcnt(0)" : "=&s"(t0) : "s"(p), "s"(q) : "memory"); }

__device__ __forceinline__ void out_tile(const v4u (&of)[4], const v2u (&bt)[8], rsrc_t rw, u32 po, u32 pubw, u32 tag) {
  f32x4 y = {0.f, 0.f, 0.f, 0.f};
#pragma unroll
  for (int i = 0; i < 4; ++i) y = mfma(of[i], v4u{bt[2 * i].x, bt[2 * i].y, bt[2 * i + 1].x, bt[2 * i + 1].y}, y);
  __builtin_amdgcn_raw_buffer_store_b128(v4u{F2U(y[0]), tag, F2U(dpp<XOR1>(y[0])), tag}, rw, po, pubw, AUX_SC1);
}
__device__ __forceinline__ u32 ps_addr(int h, int col, u32 pol) { return W_PS + (col >> 5) * 16384 + (pol ^ ((col & 128) << 6)) + h * 256 + (col & 31) * 8; }
__device__ __forceinline__ u32 slat(const void* p) {
  u64 t0, t1; u32 d;
  asm volatile("s_memtime %0\n\ts_load_dword %2, %3, 0x0\n\ts_waitcnt lgkmcnt(0)\n\ts_memtime %1\n\ts_waitcnt lgkmcnt(0)" : "=&s"(t0), "=&s"(t1), "=&s"(d) : "s"(p) : "memory");
  return (u32)t1 - (u32)t0;
}
struct Ptrs { const u16 *x, *wq, *kc, *vc, *wo, *w1, *w2; unsigned char* ws; const u16 *b1, *b2; u16* out; u64 pad; };

#define LDW(i) "global_load_dwordx4 %" #i ", %10, %11 nt\n\tv_add_u32 %10, %12, %10\n\t"
#define LDW10 LDW(0) LDW(1) LDW(2) LDW(3) LDW(4) LDW(5) LDW(6) LDW(7) LDW(8) LDW(9)
#define STR_(x) #x
#define STR(x) STR_(x)

__global__ void __launch_bounds__(NTHR, 3) __attribute__((aligned(4096))) fused_decode(Ptrs P) {
  __shared__ __attribute__((aligned(1024))) unsigned char smem[L_END];
  asm volatile("" :: "v"((LAS unsigned char*)smem) : "memory");
  v16u ka; v8u kb;
  const auto kp = __builtin_amdgcn_kernarg_segment_ptr();
  asm volatile("s_load_dwordx16 %0, %1, 0x0" : "=&s"(ka) : "s"(kp) : "memory");
  const int b = blockIdx.x, lane = threadIdx.x & 63, wv = __builtin_amdgcn_readfirstlane(threadIdx.x >> 6);
  const int xc = b & 7, c = b >> 3, h = ((c >> 3) & 1) | (xc << 1) | (c & 16), j = c & 7;
  const int la = lane >> 3, lb = lane & 7;
  u32 l16 = lane * 16, l4 = lane * 4, zero = 0, lb16 = lb * 16; PINV(l16); PINV(l4); PINV(zero); PINV(lb16);
#define KPTR(v, i) ((u64)v[2 * (i)] | ((u64)v[2 * (i) + 1] << 32))
  v4u xa, w[20]; u32 xb, epoch;
  if (wv == 0) {
    __builtin_amdgcn_s_setprio(2);
    u32 vo = (u32)(160 * (j >> 1) + 20 * la) * 8192u + (u32)(h * 128 + 64 * (j & 1)) * 2u + lb16, str = 8192; PINV(vo); PINS(str);
    asm volatile("s_waitcnt lgkmcnt(0)" : "+s"(ka) :: "memory");
    asm volatile("global_load_dwordx4 %0, %2, %4\n\tglobal_load_dword %1, %3, %4 offset:1024" : "=&v"(xa), "=&v"(xb) : "v"(l16), "v"(l4), "s"(KPTR(ka, 0)) : "memory");
    asm volatile("s_load_dword %0, %1, 0x0" : "=&s"(epoch) : "s"(KPTR(ka, 7)) : "memory");
    asm volatile(LDW10 : "=&v"(w[0]), "=&v"(w[1]), "=&v"(w[2]), "=&v"(w[3]), "=&v"(w[4]), "=&v"(w[5]), "=&v"(w[6]), "=&v"(w[7]), "=&v"(w[8]), "=&v"(w[9]), "+v"(vo) : "s"(KPTR(ka, 1)), "s"(str) : "memory");
    asm volatile(LDW10 : "=&v"(w[10]), "=&v"(w[11]), "=&v"(w[12]), "=&v"(w[13]), "=&v"(w[14]), "=&v"(w[15]), "=&v"(w[16]), "=&v"(w[17]), "=&v"(w[18]), "=&v"(w[19]), "+v"(vo) : "s"(KPTR(ka, 1)), "s"(str) : "memory");
  }
  asm volatile("s_load_dwordx8 %0, %1, 0x40" : "=&s"(kb) : "s"(kp) : "memory");
  {
    u64 pc;
    asm volatile("s_cmp_lg_u32 %[wv], 2\n\ts_cbranch_scc1 1f\n\ts_getpc_b64 %[pc]\n\ts_movk_i32 m0, %[jk]\n\ts_nop 0\n\tglobal_load_lds_dwordx4 %[vo], %[pc] offset:512\n1:"
                 : [pc] "=&s"(pc) : [wv] "s"(wv), [jk] "n"(L_JUNK - 512), [vo] "v"(l16 + ((u32)c & 7u) * 1024u) : "memory", "scc");
  }
#define ROLE_BEGIN() ROLE_BEGIN_("s_waitcnt lgkmcnt(0)\n\ts_barrier")
#define ROLE_BEGIN_(W) asm volatile(W : "+s"(ka), "+s"(kb) :: "memory"); \
  unsigned char* ws = (unsigned char*)KPTR(ka, 7); const rsrc_t rw = mk(ws, W_END)
  const f32x4 zero4 = {0.f, 0.f, 0.f, 0.f};
  u32 nsp = 1u << 28; PINS(nsp);
#define MASKS() v4u msk; { const u32 hm = (lane & 1) ? 0xffff0000u : 0x0000ffffu; const int q_ = lb >> 1; \
    msk = v4u{q_ == 0 ? hm : 0u, q_ == 1 ? hm : 0u, q_ == 2 ? hm : 0u, q_ == 3 ? hm : 0u}; PINV(msk); } \
    u32 pubo = (u32)((lane >> 4) * 128) + lb16 + ((lane & 8) ? OOB : 0u); PINV(pubo)
#define SEL(m) (v4u{(m) & msk.x, (m) & msk.y, (m) & msk.z, (m) & msk.w})
  asm volatile("s_cmp_lt_u32 %0, 7\n\ts_cbranch_scc1 2f\n\ts_cmp_lg_u32 %0, 7\n\ts_cbranch_scc1 3f\n\ts_call_b64 s[72:73], .Lfd_pad_l\n3:\n\t"
               "s_cmp_lg_u32 %0, 8\n\ts_cbranch_scc1 1f\n\ts_call_b64 s[72:73], .Lfd_pad_q\n1:\n\t"
               "s_cmp_lg_u32 %0, 9\n\ts_cbranch_scc1 2f\n\ts_call_b64 s[72:73], .Lfd_pad_a\n2:" :: "s"(wv) : "scc", "s72", "s73");
#define PAD(name) asm volatile("s_branch 1f\n" name ":\n\ts_setpc_b64 s[72:73]\n1:")
  asm goto("s_cmp_eq_u32 %0, 1\n\ts_cbranch_scc1 %l1\n\ts_cmp_eq_u32 %0, 2\n\ts_cbranch_scc1 %l2" :: "s"(wv >> 1) : "scc" : R_ATT, R_MLP);

  {
#define PC(v, off, pol) "global_load_lds_dwordx4 " v ", s[66:67] offset:" off pol "\n\t"
#define DUE(l) l ":\n\ts_memrealtime s[78:79]\n\ts_waitcnt lgkmcnt(0)\n\ts_lshl_b32 s78, s78, 4\n\ts_sub_i32 s78, s78, s74\n\ts_cmp_lt_i32 s78, 0\n\ts_cbranch_scc1 " l "b\n\t" \
               "s_addk_i32 s78, -" STR(SLACK) "\n\ts_max_i32 s78, s78, 0\n\ts_add_u32 s74, s74, s78\n\t"
#define LAND(n) "v_mov_b32 v127, 64-" #n "\n\ts_waitcnt vmcnt(" #n ")\n\tds_write_b32 v125, v127\n\t"
    asm volatile(
      "s_cmp_eq_u32 %[wv], 0\n\ts_cbranch_scc1 9f\n\t"
      "s_waitcnt lgkmcnt(0)\n\ts_memrealtime s[78:79]\n\t"
      "s_setprio 3\n\t"
      "s_movk_i32 s76, 0x2800\n\tv_mad_u32_u24 v126, %[la], s76, %[lb16]\n\t"
      "s_cmp_lg_u32 %[wv], 1\n\ts_cbranch_scc1 2f\n\t"
      "global_load_dword v119, %[z], %[x]\n\t"
      "v_mov_b32 v125, %[fs1]\n\t"
      "s_mov_b32 s76, 0x8700\n\tv_mad_u32_u24 v120, %[la], s76, %[lb16]\n\t"
      "s_movk_i32 s76, 2432\n\tv_add_u32 v120, 0x800, v120\n\tv_add_u32 v121, s76, v120\n\tv_add_u32 v122, s76, v121\n\tv_add_u32 v123, s76, v122\n\tv_add_u32 v124, s76, v123\n\t"
      "s_mul_i32 s66, %[xc], 276480\n\ts_lshl_b32 s76, %[c], 7\n\ts_add_u32 s66, s66, s76\n\ts_add_u32 s66, s66, %[w1l]\n\ts_addc_u32 s67, %[w1h], 0\n\t"
      "s_movk_i32 m0, %[lw1]+2048\n\ts_movk_i32 s70, 17280\n\ts_mov_b32 s71, 2\n\t"
      "ds_write_b32 %[l4], %[z]\n\t"
      "s_waitcnt lgkmcnt(0)\n\ts_barrier\n\t"
      "s_lshl_b32 s74, s78, 4\n\ts_addk_i32 s74, 16*" STR(L_T0) "\n\ts_waitcnt vmcnt(0)\n\t"
      "s_cmp_gt_u32 %[c], 26\n\ts_cbranch_scc1 1f\n\t"
      "s_call_b64 s[72:73], 5f\n\ts_branch 8f\n"
      DUE("5")
      PC("v120", "-2048", "") PC("v121", "-1024", "") PC("v122", "0", "") PC("v123", "1024", "") PC("v124", "2048", "")
      "s_add_u32 m0, m0, 0x1400\n\ts_add_u32 s66, s66, s70\n\ts_addc_u32 s67, s67, 0\n\ts_addk_i32 s74, 5*" STR(STEP) "\n\t"
      "s_sub_u32 s71, s71, 1\n\ts_cmp_lg_u32 s71, 0\n\ts_cbranch_scc1 5b\n\t"
      "s_setpc_b64 s[72:73]\n"
      "1:\n\ts_addk_i32 s74, 10*" STR(STEP) "\n\ts_cmpk_lt_u32 %[b], 242\n\ts_cbranch_scc1 8f\n\t"
      "s_addk_i32 s74, -8*" STR(STEP) "\n\ts_add_u32 s75, %[b], 14\n\ts_movk_i32 m0, %[lw1]\n\ts_call_b64 s[72:73], 3f\n\ts_branch 8f\n"
      "2:\n\t"
      "s_lshl_b32 s75, %[h], 20\n\ts_lshl_b32 s76, %[j], 15\n\ts_or_b32 s75, s75, s76\n\ts_or_b32 s75, s75, 0xc0000\n\t"
      "s_add_u32 s68, %[j], %[h]\n\ts_lshl_b32 s68, s68, 12\n\ts_movk_i32 s69, 0x7000\n\ts_movk_i32 s70, 0x1000\n\ts_mov_b32 s71, 8\n\t"
      "s_lshl_b32 s76, %[wv], 2\n\ts_add_u32 s76, s76, %[fs1]-20\n\tv_mov_b32 v125, s76\n\t"
      "s_add_u32 s64, %[kl], s75\n\ts_addc_u32 s65, %[kh], 0\n\t"
      "v_lshrrev_b32 v127, 4, %[l16]\n\tv_and_b32 v127, 48, v127\n\tv_xor_b32 v120, %[l16], v127\n\t"
      "v_xor_b32 v121, 64, v120\n\tv_xor_b32 v122, 0x80, v120\n\tv_xor_b32 v123, 64, v122\n\ts_movk_i32 m0, %[lw1]+10240\n\t"
      "s_barrier\n14:\n\ts_sleep 2\n\tds_read_b32 v127, v125 offset:%[nxo]-4\n\ts_waitcnt lgkmcnt(0)\n\tv_readfirstlane_b32 s74, v127\n\ts_nop 0\n\ts_cmp_eq_u32 s74, 0\n\ts_cbranch_scc1 14b\n\t"
      "s_cmp_lg_u32 %[wv], 6\n\ts_cbranch_scc1 11f\n\t"
      "s_call_b64 s[72:73], 4f\n\ts_branch 8f\n"
      "3:\n\t"
      "s_mul_i32 s76, s75, 6554\n\ts_lshr_b32 s76, s76, 16\n\ts_mul_i32 s76, s76, 80640\n\ts_lshl_b32 s75, s75, 7\n\ts_add_u32 s76, s76, s75\n\t"
      "s_add_u32 s64, %[w2l], s76\n\ts_addc_u32 s65, %[w2h], 0\n\t"
      "v_mov_b32 v120, v126\n\tv_add_u32 v121, 0x100, v126\n\tv_add_u32 v122, 0x200, v126\n\tv_add_u32 v123, 0x300, v126\n\t"
      "s_mov_b32 s68, 0\n\ts_mov_b32 s69, -1\n\ts_movk_i32 s70, 5120\n\ts_mov_b32 s71, 2\n"
      DUE("4")
      "s_and_b32 s76, s68, s69\n\ts_add_u32 s66, s64, s76\n\ts_addc_u32 s67, s65, 0\n\t"
      PC("v120", "0", " nt") PC("v121", "1024", " nt") PC("v122", "2048", " nt") PC("v123", "3072", " nt")
      "s_add_u32 m0, m0, 0x1000\n\ts_add_u32 s68, s68, s70\n\ts_addk_i32 s74, 4*" STR(STEP) "\n\t"
      "s_sub_u32 s71, s71, 1\n\ts_cmp_lg_u32 s71, 0\n\ts_cbranch_scc1 4b\n\t"
      "s_setpc_b64 s[72:73]\n"
      "11:\n\ts_cmp_lg_u32 %[wv], 7\n\ts_cbranch_scc1 12f\n\t"
      "s_add_u32 s64, %[vl], s75\n\ts_addc_u32 s65, %[vh], 0\n\ts_mov_b32 m0, %[lw1]+43008\n"
      DUE("6")
      "s_and_b32 s76, s68, s69\n\ts_add_u32 s66, s64, s76\n\ts_addc_u32 s67, s65, 0\n\t"
      PC("%[l16]", "0", " nt") "s_add_u32 m0, m0, 32\n\ts_add_u32 s68, s68, s70\n\t" PC("%[l16]", "1024", " nt") "s_add_u32 m0, m0, 32\n\ts_addk_i32 s74, 4*" STR(STEP) "\n\t"
      PC("%[l16]", "2048", " nt") "s_add_u32 m0, m0, 32\n\ts_sub_u32 s71, s71, 1\n\t" PC("%[l16]", "3072", " nt") "s_add_u32 m0, m0, 0x1020\n\t"
      "s_cmp_lg_u32 s71, 0\n\ts_cbranch_scc1 6b\n\ts_branch 8f\n"
      "12:\n\ts_cmp_lg_u32 %[wv], 8\n\ts_cbranch_scc1 13f\n\t"
      "s_lshl_b32 s75, %[xc], 5\n\ts_add_u32 s75, s75, %[c]\n\ts_mov_b32 m0, %[lw1]+76800\n\ts_call_b64 s[72:73], 3b\n\ts_branch 8f\n"
      "13:\n\t"
      "s_mul_i32 s66, %[h], 163840\n\ts_mul_i32 s76, %[j], 160\n\ts_add_u32 s66, s66, s76\n\ts_add_u32 s66, s66, %[wol]\n\ts_addc_u32 s67, %[woh], 0\n\t"
      "s_movk_i32 s75, 1120\n\tv_add_u32 v126, 0x800, %[l16]\n\tv_mul_u32_u24 v127, 410, %[l16]\n\ts_mov_b32 s76, 419840\n\t"
      "v_lshrrev_b32 v120, 16, v127\n\tv_add_u32 v127, s76, v127\n\tv_mad_u32_u24 v120, v120, s75, v126\n\t"
      "v_lshrrev_b32 v121, 16, v127\n\tv_add_u32 v127, s76, v127\n\tv_mad_u32_u24 v121, v121, s75, v126\n\t"
      "v_lshrrev_b32 v122, 16, v127\n\tv_add_u32 v127, s76, v127\n\tv_mad_u32_u24 v122, v122, s75, v126\n\t"
      "v_lshrrev_b32 v123, 16, v127\n\tv_add_u32 v127, s76, v127\n\tv_mad_u32_u24 v123, v123, s75, v126\n\t"
      "v_lshrrev_b32 v124, 16, v127\n\tv_mad_u32_u24 v124, v124, s75, v126\n\t"
      "s_mov_b32 m0, %[lw1]+84992+2048\n\ts_mov_b32 s70, 40960\n\ts_mov_b32 s71, 4\n\t"
      "s_call_b64 s[72:73], 5b\n"
      "8:\n\ts_or_b32 s74, s74, 1\n\tv_mov_b32 v127, s74\n\tds_write_b32 v125, v127 offset:%[nxo]\n\t"
      LAND(32) LAND(24) LAND(16) LAND(10) LAND(8) LAND(0)
      "10:\n\ts_cmp_lt_u32 %[wv], 8\n\ts_cbranch_scc1 9f\n\ts_endpgm\n"
      ".Lfd_pad_l:\n\ts_setpc_b64 s[72:73]\n"
      "9:"
      :: [wv] "s"(wv), [b] "s"(b), [xc] "s"(xc), [c] "s"(c), [h] "s"(h), [j] "s"(j), [x] "s"(KPTR(ka, 0)),
         [kl] "s"(ka[4]), [kh] "s"(ka[5]), [vl] "s"(ka[6]), [vh] "s"(ka[7]), [wol] "s"(ka[8]), [woh] "s"(ka[9]), [w1l] "s"(ka[10]), [w1h] "s"(ka[11]), [w2l] "s"(ka[12]), [w2h] "s"(ka[13]),
         [la] "v"(la), [lb16] "v"(lb16), [l16] "v"(l16), [l4] "v"(l4), [z] "v"(zero), [nxo] "n"(NXO), [lw1] "n"(L_W1),
         [fs1] "n"(FL_S1)
      : "memory", "scc", "s64", "s65", "s66", "s67", "s68", "s69", "s70", "s71", "s72", "s73", "s74", "s75", "s76", "s78", "s79", "s80", "s81",
        "v119", "v120", "v121", "v122", "v123", "v124", "v125", "v126", "v127");
    asm goto("s_cmp_lg_u32 %0, 0\n\ts_cbranch_scc1 %l1" :: "s"(wv) : "scc" : R_OUT);

    ROLE_BEGIN_("");
    MASKS();
    u32 maddr = L_XN + 4 * (160 * (j >> 1) + 20 * la); PINV(maddr);
    u32 pubs = W_QX + (u32)((h * 4 + (j >> 1)) * 128 + 64 * (j & 1)) * 8u; PINS(pubs);
    SB();
    asm volatile("s_barrier\n\ts_waitcnt vmcnt(20)" : "+v"(xa), "+v"(xb) :: "memory");
    {
      f32x2 v[5] = {{bflo(xa.x), bfhi(xa.x)}, {bflo(xa.y), bfhi(xa.y)}, {bflo(xa.z), bfhi(xa.z)}, {bflo(xa.w), bfhi(xa.w)}, {bflo(xb), bfhi(xb)}};
      const float piv = U2F(__builtin_amdgcn_readfirstlane(F2U(v[0].x)));
      f32x2 s1 = v[0] - piv, s2 = s1 * s1;
#pragma unroll
      for (int i = 1; i < 5; ++i) { const f32x2 d = v[i] - piv; s1 += d; s2 += d * d; }
      float r = fold32(s1.x + s1.y, s2.x + s2.y);
      r += dpp<XOR1>(r); r += dpp<XOR2>(r); r += dpp<HMIR>(r); r += dpp<ROR8>(r); r = fold16(r, r) * (1.f / D);
      auto t = __builtin_amdgcn_permlane32_swap(F2U(r), F2U(r), false, false);
      const float S1 = U2F(t[0]), S2 = U2F(t[1]);
      const float var = rbf(fmaxf(fmaf(-S1, S1, S2), 0.f));
      const float m = rbf(piv + S1);
      const float den = rbf(__builtin_amdgcn_sqrtf(rbf(var + 1e-5f)));
      const float rc = __builtin_amdgcn_rcpf(den);
      u32 xd[10];
#pragma unroll
      for (int i = 0; i < 5; ++i) {
        const f32x2 d = v[i] - m;
        const u32 dp = cvtpk(d.x, d.y);
        const f32x2 q = f32x2{bflo(dp), bfhi(dp)} * rc;
        xd[2 * i] = cvtpk(q.x, q.x); xd[2 * i + 1] = cvtpk(q.y, q.y);
      }
      lds_st<v4u>(L_XN + 2 * l16, v4u{xd[0], xd[1], xd[2], xd[3]});
      lds_st<v4u>(L_XN + 2 * l16 + 16, v4u{xd[4], xd[5], xd[6], xd[7]});
      lds_st<v2u>(L_XN + 2048 + 2 * l4, v2u{xd[8], xd[9]});
    }
    asm volatile("s_waitcnt lgkmcnt(0)" : "+s"(epoch), "+s"(kb) :: "memory");
    const u16 *x = (const u16*)KPTR(ka, 0), *b2 = (const u16*)KPTR(kb, 1); u16* out = (u16*)KPTR(kb, 2);
    const u32 tag = epoch * 0x9E3779B1u + 0x7F4A7C15u;
    lds_st<v2u>(FL_XN, v2u{1u, tag});
    CBAR();
    v4u mm[5];
#pragma unroll
    for (int i = 0; i < 5; ++i) mm[i] = lds_ld<v4u>(maddr + 16 * i);
    SB(); PAD(".Lfd_pad_q"); SB();
    f32x4 acc;
    v4u sa = SEL(mm[0][0]), sb;
#define QSTEP2(t, M0) do { sb = SEL(mm[((t) + 1) >> 2][((t) + 1) & 3]); gstep<M0, 19 - (t)>(acc, sa, w[t], sb); \
      if ((t) < 18) { sa = SEL(mm[(((t) + 2) >> 2) % 5][((t) + 2) & 3]); gstep<0, 18 - (t)>(acc, sb, w[(t) + 1], sa); } else gstep<2, 0>(acc, sb, w[(t) + 1], sa); } while (0)
    QSTEP2(0, 1); QSTEP2(2, 0); QSTEP2(4, 0); QSTEP2(6, 0); QSTEP2(8, 0); QSTEP2(10, 0); QSTEP2(12, 0);
    QSTEP2(14, 0); QSTEP2(16, 0); QSTEP2(18, 0);
    publish64(acc, rw, pubo, pubs, tag);
    {
      const u32 gs = W_QX + (u32)h * 4096u;
      u32 qa = (lane >> 5) * 128 + lb16 + (lane & 8) + ((lane >> 2) & 4); PINV(qa);
      v4u A[4];
      __builtin_amdgcn_s_sleep(Q_S2);
      lds_wait<2, FL_SK>(nsp, zero, 32);
      __builtin_amdgcn_s_sleep(Q_S1);
#pragma unroll 1
      for (int spin = 0; spin < SPIN_MAX; ++spin) {
        CBAR();
#pragma unroll
        for (int i_ = 0; i_ < 4; ++i_) A[i_] = ldx(rw, l16 + 1024 * i_, gs);
        if (all_zero(tdiff(A[0], tag) | tdiff(A[1], tag) | tdiff(A[2], tag) | tdiff(A[3], tag))) break;
      }
      lds_st<u32>(L_Q + qa, cvtpk((U2F(A[0].x) + U2F(A[1].x)) + (U2F(A[2].x) + U2F(A[3].x)), (U2F(A[0].z) + U2F(A[1].z)) + (U2F(A[2].z) + U2F(A[3].z))));
      lds_set<FL_Q>(zero);
    }
    u32 pol;
    {
      __builtin_amdgcn_s_sleep(PDELAY);
      v4u g;
#pragma unroll 1
      for (int spin = 0; spin < SPIN_MAX; ++spin) {
        CBAR();
        g = ldx(rw, zero, W_POL);
        if (all_zero(tdiff(g, tag))) break;
        __builtin_amdgcn_s_sleep(8);
      }
      pol = g.x & 8192u;
      lds_st<u32>(FL_POL, pol + 1);
    }
    if (xc + 8 * (c >> 3) >= 20) {
      if (b == 255) {
        __builtin_amdgcn_s_sleep(32);
#pragma unroll 1
        for (int spin = 0; spin < SPIN_MAX; ++spin) {
          CBAR();
          if (all_zero(tdiff(ldx(rw, lane * 512, W_QX), tag) | tdiff(ldx(rw, lane * 512, W_QX + 32768), tag) | tdiff(ldx(rw, lane * 512, W_QX + 65536), tag) | tdiff(ldx(rw, lane * 512, W_QX + 98304), tag))) break;
        }
        __builtin_amdgcn_raw_buffer_store_b32(epoch + 1u, rw, lane == 0 ? W_EP : OOB, 0, AUX_SC1);
      }
      __builtin_amdgcn_endpgm();
    }
    const int cs = 32 * (xc + 8 * (c >> 3)) + 4 * (c & 7), col0 = cs + 2 * (lane & 1);
    const v2u sx = *(const CAS v2u*)(uintptr_t)(x + cs), sb2 = *(const CAS v2u*)(uintptr_t)(b2 + cs);
    const u32 fx = (lane & 1) ? sx.y : sx.x, fb2 = (lane & 1) ? sb2.y : sb2.x;
    const int p = lane >> 1;
    u32 off = ps_addr(0, cs, pol) + p * 256 + (lane & 1) * 16;
    u32 offf = W_PF + (u32)((p < 27 ? p : 26) * 5120 + (col0 >> 6) * 512 + (col0 & 2) * 128 + (col0 & 4) * 32 + ((col0 >> 3) & 7) * 16);
    const bool hi = lane >= 32;
    float xv = hi ? bfhi(fx) : bflo(fx), bv = hi ? bfhi(fb2) : bflo(fb2);
    u32 fmask = p < 27 ? 0xffffffffu : 0u;
    u32 ooff = ((lane & 30) == 0) ? (u32)(col0 + (hi ? 1 : 0)) * 2u : OOB;
    const rsrc_t ro = mk(out, D * 2);
    PINV(off); PINV(offf); PINV(xv); PINV(bv); PINV(fmask); PINV(ooff);
#define COLSUM(r, A, B) do { r = fold32(A, B); r = fold16(r, r); r += dpp<XOR2>(r); r += dpp<ROR4>(r); r += dpp<ROR8>(r); } while (0)
    lds_wait<2, FL_SO>(nsp, zero, 64);
    __builtin_amdgcn_s_sleep(F_DELAY);
    v4u g;
#pragma unroll 1
    for (int spin = 0; spin < SPIN_MAX; ++spin) {
      CBAR();
      g = ldx(rw, offf, 0);
      if (all_zero(tdiff(g, tag))) break;
      __builtin_amdgcn_s_sleep(4);
    }
    float fs; COLSUM(fs, U2F(g.x & fmask), U2F(g.z & fmask));
    const float fv = rbf(rbf(fs) + bv);
    float gv = rbf(fv * __builtin_amdgcn_rcpf(1.f + fexp(-fv))); PINV(gv);
    lds_wait<1, FL_G>(nsp, zero, 1);
    __builtin_amdgcn_s_sleep(E3_DELAY);
    {
      v4u rwv = {ka[14], ka[15] & 0xffffu, W_END, 0x00020000u};
      u32 spin = SPIN_MAX, gx, gz;
#define PL(r) "buffer_load_dwordx4 v[" #r ":" #r "+3], %[off], %[rw], 0 offen sc1\n\t"
#define PK(r) "s_waitcnt vmcnt(2)\n\tv_bitop3_b32 v[" #r "+1], v[" #r "+1], v[" #r "+3], %[tag] bitop3:0x7e\n\tv_mov_b32 %[gx], v[" #r "]\n\tv_cmp_ne_u32 vcc, 0, v[" #r "+1]\n\tv_mov_b32 %[gz], v[" #r "+2]\n\ts_cbranch_vccz 3f\n\t"
      asm volatile(PL(96) "s_sleep " STR(E3P) "\n\t" PL(100) "s_sleep " STR(E3P) "\n"
                   "1:\n\t" PL(104) PK(96) PL(96) PK(100) PL(100) PK(104)
                   "s_sub_u32 %[n], %[n], 1\n\ts_cbranch_scc0 1b\n3:"
                   : [gx] "=&v"(gx), [gz] "=&v"(gz), [n] "+s"(spin) : [off] "v"(off), [rw] "s"(rwv), [tag] "s"(tag)
                   : "memory", "vcc", "scc", "v96", "v97", "v98", "v99", "v100", "v101", "v102", "v103", "v104", "v105", "v106", "v107");
      g.x = gx; g.z = gz;
    }
    float ss; COLSUM(ss, U2F(g.x), U2F(g.z));
    const float ov = xv + rbf(rbf(ss) * gv);
    __builtin_amdgcn_raw_buffer_store_b16((u16)cvtpk(ov, ov), ro, ooff, 0, 0);
    __builtin_amdgcn_endpgm();
  }

R_ATT: {
    ROLE_BEGIN();
    __builtin_amdgcn_s_setprio(2);
    const int hf = wv - 2;
    const int key = lane & 15, g = lane >> 4;
    u32 reco = W_OX + (u32)(h * 8 + j) * OREC + hf * 512; PINS(reco);
    u32 ka_ = L_K + hf * 16384 + key * 256 + ((g ^ key) << 4); PINV(ka_);
    u32 qa = lane & 48, h4 = hf * 4, o4 = 4 - hf * 4; PINV(qa); PINV(h4); PINV(o4);
    u32 pa = L_P + (64 * hf + 32 * (g >> 1) + 8 * ((g & 1) | (key & 2)) + 4 * (key & 1) + (key >> 2)) * 2; PINV(pa);
    u32 pa0 = (lane & 8) ? L_Z : L_P + qa, pa1 = (lane & 8) ? L_P + 128 + qa : L_Z; PINV(pa0); PINV(pa1);
    u32 va = L_V + (((lane >> 2) & 7) * VP) + (g >> 1) * 512 + hf * 128 + (lane & 3) * 8; PINV(va);
    v4u rwv = {ka[14], ka[15] & 0xffffu, W_END, 0x00020000u}; PINS(rwv);
    u32 gh = W_OX + (u32)(h * 8) * OREC + hf * 512; PINS(gh);
    u32 oo = la * OREC + lb16; PINV(oo);
    u32 oma = L_O + hf * 128 + g * 32 + lb * 4, l8 = lane * 8; PINV(oma); PINV(l8);
    const bool g1 = (g & 1) != 0, g2 = (g & 2) != 0;
    const u32 tag = lds_wait_tag<4>(nsp, zero);
    u32 da = L_WO + (8 * g + ((lane >> 2) & 3)) * 160 + 32 * hf + 8 * (lane & 3), po = (lane & 49) ? OOB : l8, l8e = (lane & 1) ? OOB : l8, obit = hf + 1; PINV(da); PINV(po); PINV(obit); PINV(l8e);
    lds_wait<2, FL_SK>(nsp, zero, 48 + 16 * hf);
    v4u kf[4][4];
#pragma unroll
    for (int ks = 0; ks < 4; ++ks)
#pragma unroll
      for (int s = 0; s < 4; ++s) kf[s][ks] = lds_ld<v4u>((ka_ ^ (ks * 64)) + s * 4096);
    v4u qf[4];
    lds_wait_rd4<FL_Q, L_Q, L_Q + 64, L_Q + 128, L_Q + 192>(nsp, zero, 1, qa, qa, qf[0], qf[1], qf[2], qf[3]);
    f32x4 sc[4] = {zero4, zero4, zero4, zero4};
#pragma unroll
    for (int ks = 0; ks < 4; ++ks)
#pragma unroll
      for (int s = 0; s < 4; ++s) sc[s] = mfma(qf[ks], kf[s][ks], sc[s]);
    float s = g2 ? (g1 ? sc[3][0] : sc[2][0]) : (g1 ? sc[1][0] : sc[0][0]);
    s = rbf(rbf(s) * 0.08838834764831845f);
    const float m = wave_max(s);
    const float p = fexp(s - m);
    lds_st<u16>(pa, (u16)cvtpk(p, p));
    lds_st<f32x2>(L_ST + 2 * h4, f32x2{m, wave_sum(p)});
    lds_set<FL_P>(h4);
    v4u pf[4];
    PAD(".Lfd_pad_a");
    lds_wait_rd4<FL_P, 0, 64, 0, 64>(nsp, o4, 1, pa0, pa1, pf[0], pf[1], pf[2], pf[3]);
    const f32x4 st = lds_ld<f32x4>(L_ST);
    f32x4 o0, o1, o2, o3;
    v2u vt[8];
#define VB(i) (v4u{vt[2 * (i)].x, vt[2 * (i)].y, vt[2 * (i) + 1].x, vt[2 * (i) + 1].y})
#define PVSTEP(ks, C0, C1, C2, C3) do { lds_wait_tr8<(ks) * 8 * VP>(nsp, zero, 40 + 8 * (ks), va, vt); \
      o0 = mfma(pf[ks], VB(0), C0); o1 = mfma(pf[ks], VB(1), C1); o2 = mfma(pf[ks], VB(2), C2); o3 = mfma(pf[ks], VB(3), C3); } while (0)
    const float M = fmx(st[0], st[2]);
    const float e0 = fexp(st[0] - M), e1 = fexp(st[2] - M), L = fmaf(e0, st[1], e1 * st[3]);
    float es = (g2 ? e1 : e0) * __builtin_amdgcn_rcpf(L); PINV(es);
    u32 tw = lane < 16 ? (tag & 0xffff0000u) | (F2U(M) >> 16) : tag; PINV(tw);
    u32 tw2 = lane < 16 ? (tag & 0xffff0000u) | ((u32)fmaf(__builtin_amdgcn_logf(L), 8192.f, 0.5f) & 0xffffu) : tag; PINV(tw2);
    SB();
    lds_wait<1, FL_SV>(nsp, zero, 40);
    PVSTEP(0, zero4, zero4, zero4, zero4);
    SB();
    lds_wait<1, FL_SV>(nsp, zero, 48);
    PVSTEP(1, o0, o1, o2, o3); SB();
    lds_wait<1, FL_SV>(nsp, zero, 56);
    PVSTEP(2, o0, o1, o2, o3); SB();
    PVSTEP(3, o0, o1, o2, o3);
    {
      const float ov = fold32((g1 ? o1[0] : o0[0]) * es, (g1 ? o3[0] : o2[0]) * es);
      __builtin_amdgcn_raw_buffer_store_b128(v4u{F2U(ov), tw, F2U(dpp<XOR1>(ov)), tw2}, rw, l8e, reco, AUX_SC1);
    }
    __builtin_amdgcn_s_sleep(O_SLEEP);
    u32 pubw = ps_addr(h, 80 * j + 16 * hf, lds_wait_pol(nsp, zero)); PINS(pubw);
    v2u bt[8];
    lds_wait<1, FL_SO>(nsp, zero, 56);
    lds_wait<0, FL_SO>(nsp, zero, 64);
    {
      u32 spin = SPIN_MAX;
#define GL4 "buffer_load_dwordx4 v[96:99], %[oo], %[rw], %[gh] offen sc1\n\tbuffer_load_dwordx4 v[100:103], %[oo], %[rw], %[gh] offen offset:128 sc1\n\t" \
            "buffer_load_dwordx4 v[104:107], %[oo], %[rw], %[gh] offen offset:256 sc1\n\tbuffer_load_dwordx4 v[108:111], %[oo], %[rw], %[gh] offen offset:384 sc1\n\t"
#define TB(i, off) "ds_read_b64_tr_b16 %" #i ", %[da] offset:" #off "\n\t"
      asm volatile(GL4
        TB(0, 0) TB(1, 640) TB(2, 5120) TB(3, 5760) TB(4, 10240) TB(5, 10880) TB(6, 15360) TB(7, 16000)
        "s_branch 2f\n"
        "1:\n\ts_sub_u32 %[n], %[n], 1\n\ts_cbranch_scc1 3f\n\t" GL4
        "2:\n\ts_waitcnt vmcnt(3)\n\t"
        "v_lshlrev_b32 v112, 16, v97\n\t"
        "v_bitop3_b32 v97, v97, v99, %[tag] bitop3:0x7e\n\tv_and_b32 v99, 0xffff, v99\n\t"
        "v_max_f32_dpp v113, v112, v112 row_ror:8 row_mask:0xf bank_mask:0xf\n\t"
        "v_cvt_f32_u32 v99, v99\n\tv_mov_b32 v114, v113\n\tv_lshrrev_b32 v97, 16, v97\n\ts_waitcnt vmcnt(2)\n\tv_bitop3_b32 v101, v101, v103, %[tag] bitop3:0x7e\n\t"
        "v_permlane16_swap_b32 v113, v114\n\tv_max_f32 v113, v113, v114\n\tv_mov_b32 v114, v113\n\t"
        "v_mul_f32 v99, 0x39000000, v99\n\ts_waitcnt vmcnt(1)\n\tv_bitop3_b32 v105, v105, v107, %[tag] bitop3:0x7e\n\t"
        "v_permlane32_swap_b32 v113, v114\n\tv_max_f32 v113, v113, v114\n\tv_sub_f32 v112, v112, v113\n\t"
        "v_fmac_f32 v99, 0x3fb8aa3b, v112\n\tv_exp_f32 v112, v99\n\t"
        "s_waitcnt vmcnt(0)\n\tv_bitop3_b32 v109, v109, v111, %[tag] bitop3:0x7e\n\t"
        "v_mul_f32 v96, v96, v112\n\tv_mul_f32 v104, v104, v112\n\t"
        "v_add_f32_dpp v113, v112, v112 row_ror:8 row_mask:0xf bank_mask:0xf\n\t"
        "v_mul_f32 v100, v100, v112\n\tv_mov_b32 v114, v113\n\tv_mul_f32 v108, v108, v112\n\t"
        "v_mul_f32 v98, v98, v112\n\tv_permlane16_swap_b32 v113, v114\n\tv_mul_f32 v106, v106, v112\n\tv_add_f32 v113, v113, v114\n\t"
        "v_mul_f32 v102, v102, v112\n\tv_mov_b32 v114, v113\n\tv_mul_f32 v110, v110, v112\n\t"
        "v_permlane32_swap_b32 v96, v104\n\tv_permlane32_swap_b32 v113, v114\n\tv_permlane32_swap_b32 v100, v108\n\tv_permlane32_swap_b32 v98, v106\n\tv_permlane32_swap_b32 v102, v110\n\t"
        "v_add_f32 v96, v96, v104\n\tv_add_f32 v100, v100, v108\n\tv_add_f32 v98, v98, v106\n\tv_add_f32 v102, v102, v110\n\t"
        "v_add_f32 v113, v113, v114\n\t"
        "v_permlane16_swap_b32 v96, v100\n\t"
        "v_rcp_f32 v113, v113\n\t"
        "v_permlane16_swap_b32 v98, v102\n\t"
        "v_add_f32 v96, v96, v100\n\tv_or3_b32 v97, v97, v101, v105\n\tv_add_f32 v98, v98, v102\n\t"
        "v_or_b32 v97, v97, v109\n\t"
        "v_add_f32_dpp v96, v96, v96 row_ror:8 row_mask:0xf bank_mask:0xf\n\t"
        "v_cmp_ne_u32 vcc, 0, v97\n\t"
        "v_add_f32_dpp v98, v98, v98 row_ror:8 row_mask:0xf bank_mask:0xf\n\t"
        "v_mul_f32 v96, v96, v113\n\tv_mul_f32 v98, v98, v113\n\t"
        "v_cvt_pk_bf16_f32 v96, v96, v98\n\t"
        "s_cbranch_vccnz 1b\n"
        "3:\n\tds_write_b32 %[oma], v96\n\tds_write_b32 %[z], %[ob] offset:%[fo]"
        : "=&v"(bt[0]), "=&v"(bt[1]), "=&v"(bt[2]), "=&v"(bt[3]), "=&v"(bt[4]), "=&v"(bt[5]), "=&v"(bt[6]), "=&v"(bt[7]), [n] "+s"(spin)
        : [oo] "v"(oo), [rw] "s"(rwv), [gh] "s"(gh), [da] "v"(da), [tag] "v"(tag), [oma] "v"(oma), [z] "v"(h4), [ob] "v"(obit), [fo] "n"(FL_O)
        : "memory", "vcc", "scc", "v96", "v97", "v98", "v99", "v100", "v101", "v102", "v103", "v104", "v105", "v106", "v107", "v108", "v109", "v110", "v111", "v112", "v113", "v114");
    }
    v4u of[4];
    lds_wait_rd4<FL_O, L_O, L_O + 64, L_O + 128, L_O + 192>(nsp, o4, 1, qa, qa, of[0], of[1], of[2], of[3]);
    out_tile(of, bt, rw, po, pubw, tag);
    lds_set<FL_G>(zero);
    __builtin_amdgcn_endpgm();
  }

R_OUT: {
    ROLE_BEGIN_("s_setprio 1");
    const int i = wv == 1 ? 2 : wv - 3;
    u32 da = L_WO + (8 * (lane >> 4) + ((lane >> 2) & 3)) * 160 + 32 * i + 8 * (lane & 3), po = (lane & 49) ? OOB : lane * 8, qa = lane & 48, fa = (lane & 1) * 4;
    PINV(da); PINV(po); PINV(qa); PINV(fa);
    const u32 pol = lds_wait_pol(nsp, zero);
    u32 tag = lds_ld<u32>(FL_TAG); PINV(tag);
    u32 pubw = ps_addr(h, 80 * j + 16 * i, pol); PINS(pubw);
    stouch4(ws + pubw, ws + ps_addr(h, 80 * j + 16 * (i & 1), pol));
    lds_wait<3, FL_SO>(nsp, zero, 64);
    v2u bt[8];
#pragma unroll
    for (int k = 0; k < 8; ++k) { bt[k] = __builtin_bit_cast(v2u, __builtin_amdgcn_ds_read_tr16_b64_v4i16((LAS v4s*)(uintptr_t)(da + (k >> 1) * 5120 + (k & 1) * 640))); }
    SB();
    __builtin_amdgcn_s_sleep(H_SLEEP);
    v4u of[4];
    lds_wait_rd4<FL_O, L_O, L_O + 64, L_O + 128, L_O + 192>(nsp, fa, 1, qa, qa, of[0], of[1], of[2], of[3]);
    out_tile(of, bt, rw, po, pubw, tag);
    __builtin_amdgcn_endpgm();
  }

R_MLP: {
    ROLE_BEGIN();
    const u16* b1 = (const u16*)KPTR(kb, 0);
    MASKS();
    const bool slot1 = wv == 4;
    u32 ma, pubs; int cnt;
    u32 tag;
    if (slot1 && c < 27) {
      ma = L_XN + 4 * (80 * xc + 10 * la); pubs = W_HX + (u32)(xc * F + 64 * c) * 8u; cnt = 5;
      tag = lds_wait_tag<2>(nsp, zero);
    } else {
      if (slot1 && b < 242) {
        if (b == 240) {
          __builtin_amdgcn_raw_buffer_store_b128(v4u{1u, 1u, 1u, 1u}, rw, la * 0x1000 + lb16, W_RACE, AUX_SC1);
          __builtin_amdgcn_s_sleep(RWAIT);
          u32 ta = ~0u, tb = ~0u;
#pragma unroll 1
          for (int k = 0; k < 2; ++k) {
            const unsigned char* p_ = ws + W_RACE + k * 0x4000;
            ta = min(ta, slat(p_)); tb = min(tb, slat(p_ + 0x2000));
          }
          const u32 tg = lds_wait_tag<2>(nsp, zero);
          const u32 pl = ta < tb ? 0u : 8192u;
          __builtin_amdgcn_raw_buffer_store_b128(v4u{pl, tg, pl, tg}, rw, lane < 8 ? l16 : OOB, W_POL, AUX_SC1);
        }
        __builtin_amdgcn_endpgm();
      }
      const int u = slot1 ? b + 14 : 32 * xc + c;
      const int fb = (u * 6554) >> 16, cb = u - 10 * fb;
      const int row = 8 * lb + ((lane >> 1) & 4) + ((lane >> 3) & 2) + (lane >> 5);
      const u32 go = W_HX + (u32)((lane >> 5) * 4 * F + 2 * (lane & 31) + 64 * fb) * 8u;
      const u32 hs = L_HS + (slot1 ? 256 : 0);
      pubs = W_PF + (u32)(fb * 640 + 64 * cb) * 8u; cnt = 4; ma = hs + 32 * la;
      u32 bias = __builtin_amdgcn_raw_buffer_load_b16(mk(b1, F * 2), (64 * fb + row) * 2, 0, 0);
      stouch8(ws + pubs);
      PINV(bias);
      tag = lds_wait_tag<2>(nsp, zero);
      lds_wait<4, FL_SK>(nsp, zero, 64);
      __builtin_amdgcn_s_sleep(H_AT);
      v4u g0, g1, g2, g3;
#pragma unroll 1
      for (int spin = 0; spin < SPIN_MAX; ++spin) {
        CBAR();
        g0 = ldx(rw, go, 0); g1 = ldx(rw, go, F * 8); g2 = ldx(rw, go, 2 * F * 8); g3 = ldx(rw, go, 3 * F * 8);
        if (all_zero(tdiff(g0, tag) | tdiff(g1, tag) | tdiff(g2, tag) | tdiff(g3, tag))) break;
      }
      const float hv = rbf(rbf(fold32((U2F(g0.x) + U2F(g1.x)) + (U2F(g2.x) + U2F(g3.x)), (U2F(g0.z) + U2F(g1.z)) + (U2F(g2.z) + U2F(g3.z)))) + bflo(bias));
      const float inner = 0.7978845608028654f * (hv + 0.044715f * hv * hv * hv);
      const float ge = hv * __builtin_amdgcn_rcpf(1.f + fexp(-2.f * inner));
      lds_st<u32>(hs + 4 * row, cvtpk(ge, ge));
    }
    u32 da = (slot1 ? L_W1 : L_W2) + l16;
    lds_wait<1, FL_S1>(nsp, slot1 ? 0u : FL_S2 - FL_S1, 64);
    f32x4 acc = zero4;
#pragma unroll 1
    do {
      const v2u mm = lds_ld<v2u>(ma);
      const v4u d0 = lds_ld<v4u>(da), d1 = lds_ld<v4u>(da + 1024);
      acc = mfma(SEL(mm.x), d0, acc);
      acc = mfma(SEL(mm.y), d1, acc);
      ma += 8; da += 2048;
    } while (--cnt);
    publish64(acc, rw, pubo, pubs, tag);
    __builtin_amdgcn_endpgm();
  }
}

extern "C" void kernel_launch(void* const* d_in, const int* in_sizes, int n_in, void* d_out, int out_size, void* d_ws, size_t ws_size,
                              hipStream_t stream) {
  Ptrs P;
  P.x = (const u16*)d_in[0]; P.kc = (const u16*)d_in[1]; P.vc = (const u16*)d_in[2]; P.wq = (const u16*)d_in[3]; P.wo = (const u16*)d_in[4];
  P.w1 = (const u16*)d_in[6]; P.b1 = (const u16*)d_in[7]; P.w2 = (const u16*)d_in[8]; P.b2 = (const u16*)d_in[9];
  P.pad = 0; P.out = (u16*)d_out; P.ws = (unsigned char*)d_ws;
  hipLaunchKernelGGL(fused_decode, dim3(256), dim3(NTHR), 0, stream, P);
}
```
